```python
import torch

def _softmax(x, axis=-1):
    return torch.softmax(x, dim=axis)

def _gelu(x):
    return torch.nn.functional.gelu(x, approximate='tanh')

def _silu(x):
    return torch.nn.functional.silu(x)

def _sigmoid(x):
    return torch.sigmoid(x)

def _layernorm(x, axis=-1, eps=1e-05):
    m = x.mean(dim=axis, keepdim=True)
    v = x.var(dim=axis, keepdim=True, unbiased=False)
    return (x - m) / torch.sqrt(v + eps)

def _scatter_add(tgt, idx, src, axis=0):
    return tgt.clone().index_add_(axis, idx, src)

def _segment_sum(src, seg, n):
    out = torch.zeros((n,) + src.shape[1:], dtype=src.dtype)
    return out.index_add_(0, seg, src)

def _pad(x, pw):
    flat = tuple((v for pair in reversed(pw) for v in pair))
    return torch.nn.functional.pad(x, flat)
kernel_name = 'adaLN_cond__sliding_block_cached__swiglu_ffn__decode'

def setup_inputs():

    _w = lambda a: (lambda t: t.to(torch.bfloat16) if t.is_floating_point() else t)(torch.as_tensor(a))

    def _A_setup():
        B, Sq, D = (8, 1, 1024)
        Hq, Hkv, Sk, Dh = (32, 4, 4096, 128)
        assert Hq % Hkv == 0, (Hq, Hkv)
        return {'x': _w((lambda *s: torch.randn(*s, dtype=torch.bfloat16) * 0.05)(B, Sq, D)), 'k_cache': _w((lambda *s: torch.randn(*s, dtype=torch.bfloat16) * 3.2)(B, Hkv, Sk, Dh)), 'v_cache': _w((lambda *s: torch.randn(*s, dtype=torch.bfloat16) * 0.4)(B, Hkv, Sk, Dh)), 'wq': _w((lambda *s: torch.randn(*s, dtype=torch.bfloat16) * 3.2)(D, Hq * Dh) * D ** (-0.5)), 'wo': _w((lambda *s: torch.randn(*s, dtype=torch.bfloat16) * 0.4)(Hq * Dh, D) * (Hq * Dh) ** (-0.5))}
    _A_in = _A_setup()

    def _B_setup():
        B, S, D, F = (8, 1, 1024, 3584)
        return {'x': _w((lambda *s: torch.randn(*s, dtype=torch.bfloat16) * 0.05)(B, S, D)), 'w_gate': _w((lambda *s: torch.randn(*s, dtype=torch.bfloat16) * 0.05)(D, F)), 'w_up': _w((lambda *s: torch.randn(*s, dtype=torch.bfloat16) * 0.05)(D, F)), 'w_down': _w((lambda *s: torch.randn(*s, dtype=torch.bfloat16) * 0.05)(F, D))}
    _B_in = _B_setup()
    return {'A_x': _A_in['x'], 'A_k_cache': _A_in['k_cache'], 'A_v_cache': _A_in['v_cache'], 'A_wq': _A_in['wq'], 'A_wo': _A_in['wo'], 'B_x': _B_in['x'], 'B_w_gate': _B_in['w_gate'], 'B_w_up': _B_in['w_up'], 'B_w_down': _B_in['w_down'], 'c': _w((lambda *s: torch.randn(*s, dtype=torch.bfloat16) * 0.1)(8, 1024) * 8.0), 'w_mod': _w((lambda *s: torch.randn(*s, dtype=torch.bfloat16) * 0.1)(1024, 6 * 1024) * 1024 ** (-0.5) * 8.0)}

def reference(A_x, A_k_cache, A_v_cache, A_wq, A_wo, B_x, B_w_gate, B_w_up, B_w_down, c, w_mod):

    def _A_ref(x, k_cache, v_cache, wq, wo):
        B, Sq, D = x.shape
        Hq, Dh = (32, 128)
        Hkv, Sk = (k_cache.shape[1], k_cache.shape[2])
        W = min(4096, max(256, Sk // 4))
        rep = Hq // Hkv
        q = (lambda x, *d: x.permute(*d))((x @ wq).reshape(B, Sq, Hq, Dh), 0, 2, 1, 3)
        k = (lambda x, n, axis: torch.repeat_interleave(x, n, dim=axis))(k_cache[:, :, Sk - W:, :], rep, axis=1)
        v = (lambda x, n, axis: torch.repeat_interleave(x, n, dim=axis))(v_cache[:, :, Sk - W:, :], rep, axis=1)
        scores = torch.einsum('bhqd,bhkd->bhqk', q, k) * Dh ** (-0.5)
        p = _softmax(scores, axis=-1)
        o = torch.einsum('bhqk,bhkd->bhqd', p, v)
        return (lambda x, *d: x.permute(*d))(o, 0, 2, 1, 3).reshape(B, Sq, Hq * Dh) @ wo

    def _B_ref(x, w_gate, w_up, w_down):
        g = _silu(x @ w_gate)
        u = x @ w_up
        return g * u @ w_down
    x = A_x
    mod = (_silu(c) @ w_mod).reshape(c.shape[0], 6, 1, -1)
    sa, ca, ga, sb, cb, gb = (mod[:, 0].clone(), mod[:, 1].clone(), mod[:, 2].clone(), mod[:, 3].clone(), mod[:, 4].clone(), mod[:, 5].clone())
    h = _layernorm(x, axis=-1) * (1 + ca) + sa
    x = x + ga * _A_ref(h, A_k_cache, A_v_cache, A_wq, A_wo)
    h = _layernorm(x, axis=-1) * (1 + cb) + sb
    x = x + gb * _B_ref(h, B_w_gate, B_w_up, B_w_down)
    return x
```

```cpp
#include <hip/hip_runtime.h>
#include <hip/hip_bf16.h>
#include <stdint.h>

typedef unsigned short u16;

__device__ __forceinline__ float bf2f(u16 b) { return __uint_as_float((unsigned)b << 16); }
__device__ __forceinline__ u16 f2bf(float f) {
  unsigned u = __float_as_uint(f);
  u += 0x7FFFu + ((u >> 16) & 1u);
  return (u16)(u >> 16);
}
__device__ __forceinline__ float rbf(float f) { return bf2f(f2bf(f)); }

constexpr int B = 8, D = 1024, HQ = 32, HKV = 4, SK = 4096, DH = 128, W = 1024, F = 3584;

__global__ void gemv8(const u16* __restrict__ a, const u16* __restrict__ w, u16* __restrict__ out, int K, int N) {
  int n = blockIdx.x * blockDim.x + threadIdx.x;
  if (n >= N) return;
  float acc[8];
  for (int m = 0; m < 8; ++m) acc[m] = 0.f;
  for (int k = 0; k < K; ++k) {
    float wv = bf2f(w[(size_t)k * N + n]);
    for (int m = 0; m < 8; ++m) acc[m] += bf2f(a[m * K + k]) * wv;
  }
  for (int m = 0; m < 8; ++m) out[m * N + n] = f2bf(acc[m]);
}

__global__ void silu_k(const u16* c, u16* o, int n) {
  int i = blockIdx.x * blockDim.x + threadIdx.x;
  if (i < n) { float x = bf2f(c[i]); o[i] = f2bf(x / (1.f + __expf(-x))); }
}

__global__ void ln_mod(const u16* x, const u16* mod, int shift_idx, int scale_idx, u16* h) {
  int b = blockIdx.x, t = threadIdx.x;
  __shared__ float red[256];
  float v[4], s = 0.f;
  for (int i = 0; i < 4; ++i) { v[i] = bf2f(x[b * D + t * 4 + i]); s += v[i]; }
  red[t] = s; __syncthreads();
  for (int o = 128; o > 0; o >>= 1) { if (t < o) red[t] += red[t + o]; __syncthreads(); }
  float mean = red[0] / D; __syncthreads();
  float q = 0.f;
  for (int i = 0; i < 4; ++i) { float d = v[i] - mean; q += d * d; }
  red[t] = q; __syncthreads();
  for (int o = 128; o > 0; o >>= 1) { if (t < o) red[t] += red[t + o]; __syncthreads(); }
  float var = rbf(red[0] / D);
  float mb = rbf(mean);
  float sd = rbf(sqrtf(rbf(var + 1e-5f)));
  for (int i = 0; i < 4; ++i) {
    int d = t * 4 + i;
    float n = rbf(rbf(v[i] - mb) / sd);
    float cs = rbf(1.f + bf2f(mod[b * 6 * D + scale_idx * D + d]));
    float r = rbf(rbf(n * cs) + bf2f(mod[b * 6 * D + shift_idx * D + d]));
    h[b * D + d] = f2bf(r);
  }
}

__global__ void attn_k(const u16* q, const u16* kc, const u16* vc, u16* o) {
  int hq = blockIdx.x, b = blockIdx.y, t = threadIdx.x;
  int hk = hq / (HQ / HKV);
  __shared__ float qs[DH], sc[W], red[256];
  if (t < DH) qs[t] = bf2f(q[b * HQ * DH + hq * DH + t]);
  __syncthreads();
  const u16* kb = kc + ((size_t)(b * HKV + hk) * SK + (SK - W)) * DH;
  const u16* vb = vc + ((size_t)(b * HKV + hk) * SK + (SK - W)) * DH;
  float mx = -INFINITY;
  for (int j = t; j < W; j += 256) {
    float s = 0.f;
    for (int d = 0; d < DH; ++d) s += qs[d] * bf2f(kb[(size_t)j * DH + d]);
    s = rbf(rbf(s) * 0.08838834764831845f);
    sc[j] = s; mx = fmaxf(mx, s);
  }
  red[t] = mx; __syncthreads();
  for (int o2 = 128; o2 > 0; o2 >>= 1) { if (t < o2) red[t] = fmaxf(red[t], red[t + o2]); __syncthreads(); }
  mx = red[0]; __syncthreads();
  float sum = 0.f;
  for (int j = t; j < W; j += 256) { float e = __expf(sc[j] - mx); sc[j] = e; sum += e; }
  red[t] = sum; __syncthreads();
  for (int o2 = 128; o2 > 0; o2 >>= 1) { if (t < o2) red[t] += red[t + o2]; __syncthreads(); }
  float inv = 1.f / red[0];
  if (t < DH) {
    float acc = 0.f;
    for (int j = 0; j < W; ++j) acc += rbf(sc[j] * inv) * bf2f(vb[(size_t)j * DH + t]);
    o[b * HQ * DH + hq * DH + t] = f2bf(acc);
  }
}

__global__ void resid(const u16* x, const u16* mod, int gidx, const u16* y, u16* xo) {
  int i = blockIdx.x * blockDim.x + threadIdx.x;
  if (i < B * D) {
    int b = i / D, d = i % D;
    float g = bf2f(mod[b * 6 * D + gidx * D + d]);
    xo[i] = f2bf(bf2f(x[i]) + rbf(g * bf2f(y[i])));
  }
}

__global__ void swiglu(const u16* g, const u16* u, u16* s) {
  int i = blockIdx.x * blockDim.x + threadIdx.x;
  if (i < B * F) {
    float x = bf2f(g[i]);
    float sg = rbf(x / (1.f + __expf(-x)));
    s[i] = f2bf(sg * bf2f(u[i]));
  }
}

extern "C" void kernel_launch(void* const* d_in, const int* in_sizes, int n_in, void* d_out, int out_size,
                              void* d_ws, size_t ws_size, hipStream_t stream) {
  const u16* x = (const u16*)d_in[0];
  const u16* kc = (const u16*)d_in[1];
  const u16* vc = (const u16*)d_in[2];
  const u16* wq = (const u16*)d_in[3];
  const u16* wo = (const u16*)d_in[4];
  const u16* wg = (const u16*)d_in[6];
  const u16* wu = (const u16*)d_in[7];
  const u16* wd = (const u16*)d_in[8];
  const u16* c = (const u16*)d_in[9];
  const u16* wm = (const u16*)d_in[10];
  u16* ws = (u16*)d_ws;
  u16* sc = ws;
  u16* mod = sc + 8192;
  u16* h = mod + 49152;
  u16* q = h + 8192;
  u16* o = q + 32768;
  u16* at = o + 32768;
  u16* x1 = at + 8192;
  u16* h2 = x1 + 8192;
  u16* g = h2 + 8192;
  u16* u = g + 28672;
  u16* s = u + 28672;
  u16* dn = s + 28672;
  silu_k<<<32, 256, 0, stream>>>(c, sc, 8192);
  gemv8<<<6144 / 64, 64, 0, stream>>>(sc, wm, mod, 1024, 6144);
  ln_mod<<<8, 256, 0, stream>>>(x, mod, 0, 1, h);
  gemv8<<<4096 / 64, 64, 0, stream>>>(h, wq, q, 1024, 4096);
  attn_k<<<dim3(32, 8), 256, 0, stream>>>(q, kc, vc, o);
  gemv8<<<1024 / 64, 64, 0, stream>>>(o, wo, at, 4096, 1024);
  resid<<<32, 256, 0, stream>>>(x, mod, 2, at, x1);
  ln_mod<<<8, 256, 0, stream>>>(x1, mod, 3, 4, h2);
  gemv8<<<3584 / 64, 64, 0, stream>>>(h2, wg, g, 1024, 3584);
  gemv8<<<3584 / 64, 64, 0, stream>>>(h2, wu, u, 1024, 3584);
  swiglu<<<112, 256, 0, stream>>>(g, u, s);
  gemv8<<<1024 / 64, 64, 0, stream>>>(s, wd, dn, 3584, 1024);
  resid<<<32, 256, 0, stream>>>(x1, mod, 5, dn, (u16*)d_out);
}
```

```cpp
#include <hip/hip_runtime.h>
#include <stdint.h>

typedef unsigned short u16;
typedef unsigned int u32;
typedef u32 v4u __attribute__((ext_vector_type(4)));
typedef u32 v2u __attribute__((ext_vector_type(2)));
typedef float f32x4 __attribute__((ext_vector_type(4)));
typedef __bf16 bf16x8 __attribute__((ext_vector_type(8)));
typedef __amdgpu_buffer_rsrc_t rsrc_t;

#define PROBE_SLEEP 2
#define DLY_X 0
#define DLY_Y 0
#define DLY_O 0

namespace {

constexpr int AUX_SC1 = 16, AUX_NT = 2, RSRC_W3 = 0x00020000;
constexpr u32 M10 = 0x3FFu, M16 = 0xFFFFu;
constexpr int SPIN_CAP = 1 << 16;

constexpr int EPOCH_OFF = 0;
constexpr int M1P_OFF = 1024;
constexpr int H_OFF = M1P_OFF + 131072;
constexpr int Q_OFF = H_OFF + 32768;
constexpr int AO_OFF = Q_OFF + 131072;
constexpr int AML_OFF = AO_OFF + 524288;
constexpr int OP_OFF = AML_OFF + 16384;
constexpr int MOD_OFF = OP_OFF + 1048576;
constexpr int X1_OFF = MOD_OFF + 131072;
constexpr int GU_OFF = X1_OFF + 32768;
constexpr int DP_OFF = GU_OFF + 229376;
constexpr int WS_BYTES = DP_OFF + 917504;
constexpr int DIAG_OFF = 4 << 20;

constexpr int L_VT = 0;
constexpr int L_RED = 0;
constexpr int L_PB = 65536;
constexpr int L_ML = L_PB + 8192;
constexpr int L_QS = L_ML + 512;
constexpr int L_ST = L_QS + 2176;
constexpr int L_SS = L_ST + 512;
constexpr int L_RED2 = L_SS + 2176;
constexpr int LDS_BYTES = L_RED2 + 17408;

struct Params {
  const u16 *x, *kc, *vc, *wq, *wo, *wg, *wu, *wd, *c, *wm;
  u16* out;
  u32* ws;
};

__device__ __forceinline__ float bf2f(u32 hi16_in_low) { return __uint_as_float(hi16_in_low << 16); }
__device__ __forceinline__ float hi2f(u32 w) { return __uint_as_float(w & 0xFFFF0000u); }
__device__ __forceinline__ float lo2f(u32 w) { return __uint_as_float(w << 16); }
typedef float f32x2 __attribute__((ext_vector_type(2)));
typedef __bf16 bf16x2 __attribute__((ext_vector_type(2)));
__device__ __forceinline__ u32 pack2(float lo, float hi) {
  return __builtin_bit_cast(u32, __builtin_convertvector(f32x2{lo, hi}, bf16x2));
}
__device__ __forceinline__ u32 rbf_bits(float f) { return pack2(0.f, f); }
__device__ __forceinline__ float rbf(float f) { return __uint_as_float(rbf_bits(f)); }
__device__ __forceinline__ float silu_f(float x) { return x / (1.f + __expf(-x)); }

__device__ __forceinline__ f32x4 mfma(v4u a, v4u b, f32x4 c) {
  return __builtin_amdgcn_mfma_f32_16x16x32_bf16(__builtin_bit_cast(bf16x8, a), __builtin_bit_cast(bf16x8, b), c, 0, 0, 0);
}
__device__ __forceinline__ rsrc_t mk_rsrc(const void* p, int bytes) {
  return __builtin_amdgcn_make_buffer_rsrc(const_cast<void*>(p), 0, bytes, RSRC_W3);
}
__device__ __forceinline__ v4u ld_nt(rsrc_t r, int voff, int soff) { return __builtin_amdgcn_raw_buffer_load_b128(r, voff, soff, AUX_NT); }
__device__ __forceinline__ v4u ld_pl(rsrc_t r, int voff, int soff) { return __builtin_amdgcn_raw_buffer_load_b128(r, voff, soff, 0); }
__device__ __forceinline__ v4u ld4(rsrc_t r, int off) { return __builtin_amdgcn_raw_buffer_load_b128(r, off, 0, AUX_SC1); }
__device__ __forceinline__ v2u ld2(rsrc_t r, int off) { return __builtin_amdgcn_raw_buffer_load_b64(r, off, 0, AUX_SC1); }
__device__ __forceinline__ u32 ld1(rsrc_t r, int off) { return __builtin_amdgcn_raw_buffer_load_b32(r, off, 0, AUX_SC1); }
__device__ __forceinline__ void st4(rsrc_t r, int off, v4u v) { __builtin_amdgcn_raw_buffer_store_b128(v, r, off, 0, AUX_SC1); }
__device__ __forceinline__ void st2(rsrc_t r, int off, v2u v) { __builtin_amdgcn_raw_buffer_store_b64(v, r, off, 0, AUX_SC1); }
__device__ __forceinline__ void st1(rsrc_t r, int off, u32 v) { __builtin_amdgcn_raw_buffer_store_b32(v, r, off, 0, AUX_SC1); }

__device__ __forceinline__ void wg_barrier() { asm volatile("s_waitcnt lgkmcnt(0)\n\ts_barrier" ::: "memory"); }
__device__ __forceinline__ void order() { asm volatile("" ::: "memory"); }
template <class T> __device__ __forceinline__ void pin(T& v) { asm volatile("" : "+v"(v)); }

__device__ __forceinline__ float dpp_ror8(float v) {
  return __int_as_float(__builtin_amdgcn_update_dpp(0, __float_as_int(v), 0x128, 0xf, 0xf, true));
}
template <int N> __device__ __forceinline__ float dpp_ror(float v) {
  return __int_as_float(__builtin_amdgcn_update_dpp(0, __float_as_int(v), 0x120 + N, 0xf, 0xf, true));
}
__device__ __forceinline__ float row16_sum(float v) {
  v += dpp_ror<8>(v); v += dpp_ror<4>(v); v += dpp_ror<2>(v); v += dpp_ror<1>(v);
  return v;
}
__device__ __forceinline__ float x16_sum(float v) {
  auto r = __builtin_amdgcn_permlane16_swap(__float_as_uint(v), __float_as_uint(v), false, false);
  return __uint_as_float(r[0]) + __uint_as_float(r[1]);
}
__device__ __forceinline__ float x32_sum(float v) {
  auto r = __builtin_amdgcn_permlane32_swap(__float_as_uint(v), __float_as_uint(v), false, false);
  return __uint_as_float(r[0]) + __uint_as_float(r[1]);
}
__device__ __forceinline__ float x16_max(float v) {
  auto r = __builtin_amdgcn_permlane16_swap(__float_as_uint(v), __float_as_uint(v), false, false);
  return fmaxf(__uint_as_float(r[0]), __uint_as_float(r[1]));
}
__device__ __forceinline__ float x32_max(float v) {
  auto r = __builtin_amdgcn_permlane32_swap(__float_as_uint(v), __float_as_uint(v), false, false);
  return fmaxf(__uint_as_float(r[0]), __uint_as_float(r[1]));
}
__device__ __forceinline__ float wave_sum(float v) { return x32_sum(x16_sum(row16_sum(v))); }

__device__ __forceinline__ u32 tag10(float v, u32 T) { return (__float_as_uint(v) & ~M10) | (T & M10); }
__device__ __forceinline__ u32 tag16(u32 bf_hi, u32 T) { return (bf_hi & ~M16) | (T & M16); }
__device__ __forceinline__ float untag10(u32 w) { return __uint_as_float(w & ~M10); }
__device__ __forceinline__ u32 bad4(v4u v, u32 T) { return (v.x ^ T) | (v.y ^ T) | (v.z ^ T) | (v.w ^ T); }
__device__ __forceinline__ u32 bad2(v2u v, u32 T) { return (v.x ^ T) | (v.y ^ T); }
__device__ __forceinline__ bool all_ok(u32 bad, u32 mask) { return __builtin_amdgcn_ballot_w64((bad & mask) != 0u) == 0ull; }
__device__ __forceinline__ void probe(rsrc_t ws, int off, u32 T, u32 mask) {
  for (int s = 0; s < SPIN_CAP; ++s) {
    u32 v = ld1(ws, off);
    if (((__builtin_amdgcn_readfirstlane(v) ^ T) & mask) == 0u) break;
    __builtin_amdgcn_s_sleep(PROBE_SLEEP);
    order();
  }
}
__device__ __forceinline__ void delay_ticks(int ticks) {
  if (ticks <= 0) return;
  const unsigned long long t0 = __builtin_amdgcn_s_memrealtime();
  while (__builtin_amdgcn_s_memrealtime() - t0 < (unsigned long long)ticks) __builtin_amdgcn_s_sleep(1);
}
__device__ __forceinline__ v4u frag8(v4u a, v4u b) {
  v4u f;
  f.x = __builtin_amdgcn_perm(a.y, a.x, 0x07060302u);
  f.y = __builtin_amdgcn_perm(a.w, a.z, 0x07060302u);
  f.z = __builtin_amdgcn_perm(b.y, b.x, 0x07060302u);
  f.w = __builtin_amdgcn_perm(b.w, b.z, 0x07060302u);
  return f;
}

__device__ __forceinline__ void transpose8(const v4u* in, v4u* out) {
#pragma unroll
  for (int t = 0; t < 8; ++t) {
    const u32 sel = (t & 1) ? 0x07060302u : 0x05040100u;
#pragma unroll
    for (int d = 0; d < 4; ++d) out[t][d] = __builtin_amdgcn_perm(in[2 * d + 1][t >> 1], in[2 * d][t >> 1], sel);
  }
}

template <int NG>
__device__ __forceinline__ void load_tile(v4u* w, rsrc_t r, int voff, int row_bytes) {
#pragma unroll
  for (int G = 0; G < NG; ++G)
#pragma unroll
    for (int i = 0; i < 8; ++i) w[G * 8 + i] = ld_nt(r, voff, (64 * G + i) * row_bytes);
}
template <int NG>
__device__ __forceinline__ void prep_tile(const v4u* w, v4u* b) {
#pragma unroll
  for (int G = 0; G < NG; ++G) transpose8(w + 8 * G, b + 8 * G);
#pragma unroll
  for (int i = 0; i < NG * 8; ++i) pin(b[i]);
}
template <int NG>
__device__ __forceinline__ void tile_mma(const v4u* a, const v4u* b, f32x4* f, bool lo8) {
  f32x4 acc[8];
#pragma unroll
  for (int t = 0; t < 8; ++t) {
    acc[t] = mfma(a[0], b[t], f32x4{0.f, 0.f, 0.f, 0.f});
    if (NG == 2) acc[t] = mfma(a[1], b[8 + t], acc[t]);
  }
  float pa[4][4], pb[4][4];
#pragma unroll
  for (int t = 0; t < 4; ++t)
#pragma unroll
    for (int r = 0; r < 4; ++r) {
      auto s = __builtin_amdgcn_permlane32_swap(__float_as_uint(acc[t][r]), __float_as_uint(acc[t + 4][r]), false, false);
      pa[t][r] = __uint_as_float(s[0]);
      pb[t][r] = __uint_as_float(s[1]);
    }
#pragma unroll
  for (int rp = 0; rp < 2; ++rp)
#pragma unroll
    for (int t = 0; t < 4; ++t) {
      float x = lo8 ? pa[t][2 * rp] : pb[t][2 * rp + 1];
      float y = lo8 ? pa[t][2 * rp + 1] : pb[t][2 * rp];
      f[rp][t] = x + dpp_ror8(y);
    }
}

typedef __attribute__((address_space(3))) unsigned char lds_t;
__device__ __forceinline__ void lds_st4(lds_t* L, int off, v4u v) { *(__attribute__((address_space(3))) v4u*)(L + off) = v; }
__device__ __forceinline__ v4u lds_ld4(lds_t* L, int off) { return *(__attribute__((address_space(3))) v4u*)(L + off); }
__device__ __forceinline__ void lds_st2(lds_t* L, int off, v2u v) { *(__attribute__((address_space(3))) v2u*)(L + off) = v; }
__device__ __forceinline__ v2u lds_ld2(lds_t* L, int off) { return *(__attribute__((address_space(3))) v2u*)(L + off); }
__device__ __forceinline__ void lds_st1(lds_t* L, int off, u32 v) { *(__attribute__((address_space(3))) u32*)(L + off) = v; }
__device__ __forceinline__ float lds_ldf(lds_t* L, int off) { return *(__attribute__((address_space(3))) float*)(L + off); }

__device__ __forceinline__ float cross_wave(lds_t* L, int red, const f32x4* f, int w, int lane) {
  const int q4 = lane >> 4, hj = (lane >> 3) & 1, c = lane & 7;
  const int base = red + w * 2176 + (4 * (q4 & 1) + hj) * 272 + (8 * c + 4 * (q4 >> 1)) * 4;
  lds_st4(L, base, __builtin_bit_cast(v4u, f[0]));
  lds_st4(L, base + 544, __builtin_bit_cast(v4u, f[1]));
  wg_barrier();
  float s = 0.f;
#pragma unroll
  for (int ww = 0; ww < 8; ++ww) s += lds_ldf(L, red + ww * 2176 + w * 272 + lane * 4);
  return s;
}
__device__ __forceinline__ void publish_part(rsrc_t ws, int off, int row_bytes, const f32x4* f, u32 T, int lane) {
  const int q4 = lane >> 4, hj = (lane >> 3) & 1, c = lane & 7;
  const int o = off + (4 * (q4 & 1) + hj) * row_bytes + (8 * c + 4 * (q4 >> 1)) * 4;
#pragma unroll
  for (int rp = 0; rp < 2; ++rp)
    st4(ws, o + 2 * rp * row_bytes, v4u{tag10(f[rp][0], T), tag10(f[rp][1], T), tag10(f[rp][2], T), tag10(f[rp][3], T)});
}
__device__ __forceinline__ v4u silu_frag(v4u cv) {
  v4u f;
#pragma unroll
  for (int d = 0; d < 4; ++d) f[d] = pack2(silu_f(lo2f(cv[d])), silu_f(hi2f(cv[d])));
  return f;
}

#define STAMP(i) do { } while (0)

template <int NP>
__device__ __forceinline__ f32x4 reduce_parts(rsrc_t ws, int off, int off0, u32 T, int lane) {
  const int hl = lane & 15;
  const int o0 = off + hl * 32768, o1 = off + (hl + 16 < NP ? hl + 16 : hl) * 32768;
  v4u a, b;
  probe(ws, off0, T, M10);
  for (int s = 0; s < SPIN_CAP; ++s) {
    a = ld4(ws, o0);
    b = ld4(ws, o1);
    if (all_ok(bad4(a, T) | bad4(b, T), M10)) break;
    order();
  }
  f32x4 r;
#pragma unroll
  for (int k = 0; k < 4; ++k) {
    float v = untag10(a[k]) + (hl + 16 < NP ? untag10(b[k]) : 0.f);
    r[k] = row16_sum(v);
  }
  return r;
}

__global__ void __launch_bounds__(512, 2) fused_block(Params p) {
  __shared__ __attribute__((aligned(16))) unsigned char lds_raw[LDS_BYTES];
  lds_t* L = (lds_t*)lds_raw;
  const int tid = threadIdx.x, lane = tid & 63, w = __builtin_amdgcn_readfirstlane(tid >> 6);
  const int bid = blockIdx.x, xl = bid & 7, u = bid >> 3;
  const int g = lane >> 4, rho = (lane >> 3) & 1, c = lane & 7, tok = lane & 7;
  const bool lo8 = rho == 0;
  const rsrc_t ws = mk_rsrc(p.ws, WS_BYTES);
  STAMP(0);
  const u32 T = __builtin_amdgcn_readfirstlane(__hip_atomic_load(p.ws, __ATOMIC_RELAXED, __HIP_MEMORY_SCOPE_AGENT)) + 1u;
  const int krow = 32 * rho + 8 * g;

  if (xl < 4 && u < 16) {
    const int zi = xl * 16 + u;
    v4u wr[16], wb[16], a[2];
    f32x4 f[2];
    {
      const int j = u, pq = xl;
      const int ct = (pq >> 1) * 16 + j, k0 = 512 * (pq & 1) + 64 * w;
      const rsrc_t rm = mk_rsrc(p.wm, 1024 * 6144 * 2);
      load_tile<1>(wr, rm, ((k0 + krow) * 6144 + 64 * ct + 8 * c) * 2, 6144 * 2);
      const rsrc_t rc = mk_rsrc(p.c, 8192 * 2), rx = mk_rsrc(p.x, 8192 * 2);
      v4u cv = ld_pl(rc, (tok * 1024 + k0 + krow) * 2, 0);
      v4u x0 = ld_pl(rx, (w * 1024 + lane * 16) * 2, 0), x1v = ld_pl(rx, (w * 1024 + lane * 16 + 8) * 2, 0);
      const int hc = 64 * j + 16 * pq + (lane & 15);
      u32 xs = __builtin_amdgcn_raw_buffer_load_b16(rx, (w * 1024 + hc) * 2, 0, 0);
      a[0] = silu_frag(cv);
      float sum = 0.f;
#pragma unroll
      for (int d = 0; d < 4; ++d) sum += (lo2f(x0[d]) + hi2f(x0[d])) + (lo2f(x1v[d]) + hi2f(x1v[d]));
      const float mean = wave_sum(sum) * (1.f / 1024.f);
      float sq = 0.f;
#pragma unroll
      for (int d = 0; d < 4; ++d) {
        float e0 = lo2f(x0[d]) - mean, e1 = hi2f(x0[d]) - mean, e2 = lo2f(x1v[d]) - mean, e3 = hi2f(x1v[d]) - mean;
        sq += (e0 * e0 + e1 * e1) + (e2 * e2 + e3 * e3);
      }
      const float var = rbf(wave_sum(sq) * (1.f / 1024.f));
      const float sd = rbf(sqrtf(rbf(var + 1e-5f)));
      const float nrm = rbf(rbf(bf2f(xs) - rbf(mean)) / sd);
      prep_tile<1>(wr, wb);
      STAMP(1);
      tile_mma<1>(a, wb, f, lo8);
      float s = cross_wave(L, L_RED, f, w, lane);
      st1(ws, M1P_OFF + (((j * 4 + pq) * 8 + w) * 64 + lane) * 4, tag10(s, T));
      STAMP(2);
      const int mo = M1P_OFF + ((j * 4 * 8 + w) * 64 + 16 * pq + (lane & 15)) * 4;
      u32 m0, m1, m2, m3;
      probe(ws, M1P_OFF + ((j * 4 * 8 + w) * 64 + 16 * pq) * 4 + ((pq + 1) & 3) * 2048, T, M10);
      for (int sp = 0; sp < SPIN_CAP; ++sp) {
        m0 = ld1(ws, mo); m1 = ld1(ws, mo + 2048); m2 = ld1(ws, mo + 4096); m3 = ld1(ws, mo + 6144);
        if (all_ok((m0 ^ T) | (m1 ^ T) | (m2 ^ T) | (m3 ^ T), M10)) break;
        order();
      }
      const float sa = rbf(untag10(m0) + untag10(m1)), ca = rbf(untag10(m2) + untag10(m3));
      const u32 hb = rbf_bits(rbf(nrm * rbf(1.f + ca)) + sa);
      if (lane < 16) st1(ws, H_OFF + (w * 1024 + hc) * 4, tag16(hb, T));
      STAMP(3);
    }
    order();
    {
      const int hh = zi >> 1, half = zi & 1;
      delay_ticks(DLY_O);
      const rsrc_t ro = mk_rsrc(p.wo, 4096 * 1024 * 2);
      load_tile<2>(wr, ro, ((128 * hh + krow) * 1024 + 512 * half + 64 * w + 8 * c) * 2, 1024 * 2);
      prep_tile<2>(wr, wb);
      STAMP(4);
      const int ao = AO_OFF + (((w * 32 + hh) * 4) * 128 + 2 * lane) * 4, ml = AML_OFF + (w * 32 + hh) * 64;
      v2u o0, o1, o2, o3; v4u ma, mb, mc, md;
      probe(ws, ml, T, M16);
      for (int sp = 0; sp < SPIN_CAP; ++sp) {
        o0 = ld2(ws, ao); o1 = ld2(ws, ao + 512); o2 = ld2(ws, ao + 1024); o3 = ld2(ws, ao + 1536);
        ma = ld4(ws, ml); mb = ld4(ws, ml + 16); mc = ld4(ws, ml + 32); md = ld4(ws, ml + 48);
        const u32 bm = (ma.x ^ T) | (ma.z ^ T) | (mb.x ^ T) | (mb.z ^ T) | (mc.x ^ T) | (mc.z ^ T) | (md.x ^ T) | (md.z ^ T);
        const u32 bl = (ma.y ^ T) | (ma.w ^ T) | (mb.y ^ T) | (mb.w ^ T) | (mc.y ^ T) | (mc.w ^ T) | (md.y ^ T) | (md.w ^ T);
        if (all_ok(((bad2(o0, T) | bad2(o1, T) | bad2(o2, T) | bad2(o3, T) | bl) & M10) | (bm & M16), M16)) break;
        order();
      }
      STAMP(5);
      const float m0 = hi2f(ma.x), m1 = hi2f(mb.x), m2 = hi2f(mc.x), m3 = hi2f(md.x);
      const float M = fmaxf(fmaxf(m0, m1), fmaxf(m2, m3));
      const float e0 = __expf(m0 - M), e1 = __expf(m1 - M), e2 = __expf(m2 - M), e3 = __expf(m3 - M);
      const float inv = 1.f / (e0 * untag10(ma.y) + e1 * untag10(mb.y) + e2 * untag10(mc.y) + e3 * untag10(md.y));
      const float oa = (e0 * untag10(o0.x) + e1 * untag10(o1.x) + e2 * untag10(o2.x) + e3 * untag10(o3.x)) * inv;
      const float ob = (e0 * untag10(o0.y) + e1 * untag10(o1.y) + e2 * untag10(o2.y) + e3 * untag10(o3.y)) * inv;
      lds_st1(L, L_QS + w * 272 + lane * 4, pack2(oa, ob));
      wg_barrier();
      a[0] = lds_ld4(L, L_QS + tok * 272 + krow * 2);
      a[1] = lds_ld4(L, L_QS + tok * 272 + (64 + krow) * 2);
      tile_mma<2>(a, wb, f, lo8);
      publish_part(ws, OP_OFF + hh * 32768 + (512 * half + 64 * w) * 4, 4096, f, T, lane);
      STAMP(6);
    }
    order();
    if (zi < 56) {
      const int ku = zi >> 1, half = zi & 1;
      const rsrc_t rd = mk_rsrc(p.wd, 3584 * 1024 * 2);
      load_tile<2>(wr, rd, ((128 * ku + krow) * 1024 + 512 * half + 64 * w + 8 * c) * 2, 1024 * 2);
      prep_tile<2>(wr, wb);
      STAMP(7);
      const int go = GU_OFF + (w * 7168 + 128 * ku + 2 * lane) * 4;
      v2u gg, uu;
      probe(ws, GU_OFF + (w * 7168 + 128 * ku) * 4, T, M16);
      for (int sp = 0; sp < SPIN_CAP; ++sp) {
        gg = ld2(ws, go); uu = ld2(ws, go + 3584 * 4);
        if (all_ok(bad2(gg, T) | bad2(uu, T), M16)) break;
        order();
      }
      STAMP(8);
      const float s0 = rbf(silu_f(hi2f(gg.x))) * hi2f(uu.x), s1 = rbf(silu_f(hi2f(gg.y))) * hi2f(uu.y);
      lds_st1(L, L_SS + w * 272 + lane * 4, pack2(s0, s1));
      wg_barrier();
      a[0] = lds_ld4(L, L_SS + tok * 272 + krow * 2);
      a[1] = lds_ld4(L, L_SS + tok * 272 + (64 + krow) * 2);
      tile_mma<2>(a, wb, f, lo8);
      publish_part(ws, DP_OFF + ku * 32768 + (512 * half + 64 * w) * 4, 4096, f, T, lane);
      STAMP(9);
    }
  } else if (xl < 4) {
    const int t = xl * 16 + (u - 16);
    v4u wr[16], wb[16], a[2], ac[2];
    f32x4 f[2];
    delay_ticks(DLY_X);
    const rsrc_t rq = mk_rsrc(p.wq, 1024 * 4096 * 2);
    load_tile<2>(wr, rq, ((128 * w + krow) * 4096 + 64 * t + 8 * c) * 2, 4096 * 2);
    const rsrc_t rc = mk_rsrc(p.c, 8192 * 2), rx = mk_rsrc(p.x, 8192 * 2);
    v4u cv0 = ld_pl(rc, (tok * 1024 + 128 * w + krow) * 2, 0), cv1 = ld_pl(rc, (tok * 1024 + 128 * w + 64 + krow) * 2, 0);
    const int rcol = 16 * t + 4 * (lane >> 4);
    v2u xv = __builtin_amdgcn_raw_buffer_load_b64(rx, (w * 1024 + rcol) * 2, 0, 0);
    ac[0] = silu_frag(cv0); ac[1] = silu_frag(cv1);
    pin(ac[0]); pin(ac[1]);
    prep_tile<2>(wr, wb);
    STAMP(1);
    {
      const int ho = H_OFF + (tok * 1024 + 128 * w + krow) * 4;
      v4u h0, h1, h2, h3;
      probe(ws, H_OFF + (w * 1024 + 128 * w) * 4, T, M16);
      for (int sp = 0; sp < SPIN_CAP; ++sp) {
        h0 = ld4(ws, ho); h1 = ld4(ws, ho + 16); h2 = ld4(ws, ho + 256); h3 = ld4(ws, ho + 272);
        if (all_ok(bad4(h0, T) | bad4(h1, T) | bad4(h2, T) | bad4(h3, T), M16)) break;
        order();
      }
      STAMP(2);
      a[0] = frag8(h0, h1); a[1] = frag8(h2, h3);
    }
    const rsrc_t rm = mk_rsrc(p.wm, 1024 * 6144 * 2);
    load_tile<2>(wr, rm, ((128 * w + krow) * 6144 + 2048 + 64 * t + 8 * c) * 2, 6144 * 2);
    order();
    tile_mma<2>(a, wb, f, lo8);
    {
      float s = cross_wave(L, L_RED, f, w, lane);
      st1(ws, Q_OFF + (w * 4096 + 64 * t + lane) * 4, tag16(rbf_bits(s), T));
    }
    STAMP(3);
    prep_tile<2>(wr, wb);
    tile_mma<2>(ac, wb, f, lo8);
    {
      float s = cross_wave(L, L_RED2, f, w, lane);
      st1(ws, MOD_OFF + (w * 4096 + 64 * t + lane) * 4, tag16(rbf_bits(s), T));
    }
    STAMP(4);
    v4u gav, gbv;
    probe(ws, MOD_OFF + (w * 4096 + 16 * t) * 4, T, M16);
    for (int sp = 0; sp < SPIN_CAP; ++sp) {
      gav = ld4(ws, MOD_OFF + (w * 4096 + rcol) * 4);
      if (all_ok(bad4(gav, T), M16)) break;
      order();
    }
    STAMP(5);
    f32x4 at = reduce_parts<32>(ws, OP_OFF + (w * 1024 + rcol) * 4, OP_OFF + (w * 1024 + 16 * t) * 4, T, lane);
    STAMP(6);
    float x1[4];
    x1[0] = rbf(lo2f(xv.x) + rbf(hi2f(gav.x) * rbf(at[0])));
    x1[1] = rbf(hi2f(xv.x) + rbf(hi2f(gav.y) * rbf(at[1])));
    x1[2] = rbf(lo2f(xv.y) + rbf(hi2f(gav.z) * rbf(at[2])));
    x1[3] = rbf(hi2f(xv.y) + rbf(hi2f(gav.w) * rbf(at[3])));
    if ((lane & 15) == 0)
      st4(ws, X1_OFF + (w * 1024 + rcol) * 4,
          v4u{tag16(__float_as_uint(x1[0]), T), tag16(__float_as_uint(x1[1]), T), tag16(__float_as_uint(x1[2]), T), tag16(__float_as_uint(x1[3]), T)});
    STAMP(7);
    for (int sp = 0; sp < SPIN_CAP; ++sp) {
      gbv = ld4(ws, MOD_OFF + (w * 4096 + 3072 + rcol) * 4);
      if (all_ok(bad4(gbv, T), M16)) break;
      order();
    }
    f32x4 dn = reduce_parts<28>(ws, DP_OFF + (w * 1024 + rcol) * 4, DP_OFF + (w * 1024 + 16 * t) * 4, T, lane);
    STAMP(8);
    if ((lane & 15) == 0) {
      v2u o;
      o.x = pack2(x1[0] + rbf(hi2f(gbv.x) * rbf(dn[0])), x1[1] + rbf(hi2f(gbv.y) * rbf(dn[1])));
      o.y = pack2(x1[2] + rbf(hi2f(gbv.z) * rbf(dn[2])), x1[3] + rbf(hi2f(gbv.w) * rbf(dn[3])));
      *(v2u*)(p.out + w * 1024 + rcol) = o;
    }
    if (t == 0 && tid == 0) p.ws[0] = T;
    STAMP(9);
  } else {
    const int yi = (xl - 4) * 32 + u;
    delay_ticks(DLY_Y);
    v4u wr[16], wb[16], a[2];
    f32x4 f[2];
    {
      const int pi = yi >> 2, sp_ = yi & 3, b = pi >> 2, hkv = pi & 3;
      const int n = lane & 15;
      const int kbase = (pi * 4096 + 3072 + 256 * sp_ + 32 * w) * 256;
      const rsrc_t rk = mk_rsrc(p.kc, 8 * 4 * 4096 * 128 * 2), rv = mk_rsrc(p.vc, 8 * 4 * 4096 * 128 * 2);
#pragma unroll
      for (int T2 = 0; T2 < 2; ++T2)
#pragma unroll
        for (int kk = 0; kk < 4; ++kk) wr[T2 * 4 + kk] = ld_nt(rk, kbase + n * 256 + g * 16, T2 * 4096 + kk * 64);
#pragma unroll
      for (int i = 0; i < 8; ++i) wr[8 + i] = ld_nt(rv, kbase + 4 * g * 256 + n * 16, (16 * (i >> 2) + (i & 3)) * 256);
      transpose8(wr + 8, wb);
#pragma unroll
      for (int t = 0; t < 8; ++t) lds_st4(L, L_VT + ((w * 4 + g) * 128 + t * 16 + (n ^ (2 * t))) * 16, wb[t]);
      wg_barrier();
#pragma unroll
      for (int v = 0; v < 8; ++v) wb[v] = lds_ld4(L, L_VT + ((v * 4 + g) * 128 + (n & 7) * 16 + ((2 * w + (n >> 3)) ^ (2 * (n & 7)))) * 16);
#pragma unroll
      for (int i = 0; i < 8; ++i) { pin(wb[i]); pin(wr[i]); }
      STAMP(1);
      {
        const int qo = Q_OFF + (b * 4096 + 1024 * hkv + 2 * tid) * 4;
        v2u qv;
        probe(ws, Q_OFF + (b * 4096 + 1024 * hkv + 128 * w) * 4, T, M16);
        for (int sp = 0; sp < SPIN_CAP; ++sp) {
          qv = ld2(ws, qo);
          if (all_ok(bad2(qv, T), M16)) break;
          order();
        }
        STAMP(2);
        lds_st1(L, L_QS + w * 272 + lane * 4, __builtin_amdgcn_perm(qv.y, qv.x, 0x07060302u));
      }
      wg_barrier();
      f32x4 sc[2];
      {
        v4u qf[4];
#pragma unroll
        for (int kk = 0; kk < 4; ++kk) qf[kk] = lds_ld4(L, L_QS + (n & 7) * 272 + kk * 64 + g * 16);
#pragma unroll
        for (int T2 = 0; T2 < 2; ++T2) {
          sc[T2] = mfma(wr[T2 * 4], qf[0], f32x4{0.f, 0.f, 0.f, 0.f});
#pragma unroll
          for (int kk = 1; kk < 4; ++kk) sc[T2] = mfma(wr[T2 * 4 + kk], qf[kk], sc[T2]);
        }
      }
      float mx = -INFINITY;
#pragma unroll
      for (int T2 = 0; T2 < 2; ++T2)
#pragma unroll
        for (int r = 0; r < 4; ++r) { sc[T2][r] = rbf(rbf(sc[T2][r]) * 0.08838834764831845f); mx = fmaxf(mx, sc[T2][r]); }
      mx = x32_max(x16_max(mx));
      float ls = 0.f;
#pragma unroll
      for (int T2 = 0; T2 < 2; ++T2)
#pragma unroll
        for (int r = 0; r < 4; ++r) { sc[T2][r] = __expf(sc[T2][r] - mx); ls += sc[T2][r]; }
      ls = x32_sum(x16_sum(ls));
      lds_st4(L, L_PB + (w * 64 + lane) * 16,
              v4u{pack2(sc[0][0], sc[0][1]), pack2(sc[0][2], sc[0][3]), pack2(sc[1][0], sc[1][1]), pack2(sc[1][2], sc[1][3])});
      if (lane < 8) lds_st2(L, L_ML + (lane * 8 + w) * 8, v2u{__float_as_uint(mx), __float_as_uint(ls)});
      wg_barrier();
      float mv[8], lv[8];
#pragma unroll
      for (int k = 0; k < 4; ++k) {
        v4u q = lds_ld4(L, L_ML + (n & 7) * 64 + k * 16);
        mv[2 * k] = __uint_as_float(q.x); lv[2 * k] = __uint_as_float(q.y);
        mv[2 * k + 1] = __uint_as_float(q.z); lv[2 * k + 1] = __uint_as_float(q.w);
      }
      float M = mv[0];
#pragma unroll
      for (int v = 1; v < 8; ++v) M = fmaxf(M, mv[v]);
      float Ls = 0.f;
      f32x4 o = {0.f, 0.f, 0.f, 0.f};
#pragma unroll
      for (int v = 0; v < 8; ++v) {
        const float e = __expf(mv[v] - M);
        Ls += e * lv[v];
        f32x4 d = mfma(wb[v], lds_ld4(L, L_PB + (v * 64 + lane) * 16), f32x4{0.f, 0.f, 0.f, 0.f});
        o += e * d;
      }
      if (n < 8) {
        const int hq = b * 32 + 8 * hkv + n;
        st4(ws, AO_OFF + ((hq * 4 + sp_) * 128 + 16 * w + 4 * g) * 4, v4u{tag10(o[0], T), tag10(o[1], T), tag10(o[2], T), tag10(o[3], T)});
        if (w == 0 && g == 0) {
          const u32 mt = tag16(__float_as_uint(M), T), lt = tag10(Ls, T);
          st4(ws, AML_OFF + (hq * 4 + sp_) * 16, v4u{mt, lt, mt, lt});
        }
      }
      STAMP(3);
    }
    order();
    const int xi = xl - 4;
    if (u < 28) {
      const int gt = xi * 28 + u, mat = gt >= 56, ct = gt - 56 * mat;
      const rsrc_t rg = mk_rsrc(mat ? p.wu : p.wg, 1024 * 3584 * 2);
      load_tile<2>(wr, rg, ((128 * w + krow) * 3584 + 64 * ct + 8 * c) * 2, 3584 * 2);
      prep_tile<2>(wr, wb);
      STAMP(4);
      float cs[16], sb[16];
      {
        const int so = MOD_OFF + (tok * 4096 + 1024 + 128 * w + krow) * 4;
        v4u s[4], k[4];
        probe(ws, MOD_OFF + (w * 4096 + 2048 + 128 * w) * 4, T, M16);
        for (int sp = 0; sp < SPIN_CAP; ++sp) {
          s[0] = ld4(ws, so); s[1] = ld4(ws, so + 16); s[2] = ld4(ws, so + 256); s[3] = ld4(ws, so + 272);
          k[0] = ld4(ws, so + 4096); k[1] = ld4(ws, so + 4112); k[2] = ld4(ws, so + 4352); k[3] = ld4(ws, so + 4368);
          if (all_ok(bad4(s[0], T) | bad4(s[1], T) | bad4(s[2], T) | bad4(s[3], T) | bad4(k[0], T) | bad4(k[1], T) | bad4(k[2], T) | bad4(k[3], T), M16)) break;
          order();
        }
#pragma unroll
        for (int i = 0; i < 16; ++i) { sb[i] = hi2f(s[i >> 2][i & 3]); cs[i] = rbf(1.f + hi2f(k[i >> 2][i & 3])); pin(sb[i]); pin(cs[i]); }
      }
      STAMP(5);
      float xf[16];
      {
        const int xo = X1_OFF + (tok * 1024 + 128 * w + krow) * 4;
        v4u xq[4];
        probe(ws, X1_OFF + (w * 1024 + 128 * w) * 4, T, M16);
        for (int sp = 0; sp < SPIN_CAP; ++sp) {
          xq[0] = ld4(ws, xo); xq[1] = ld4(ws, xo + 16); xq[2] = ld4(ws, xo + 256); xq[3] = ld4(ws, xo + 272);
          if (all_ok(bad4(xq[0], T) | bad4(xq[1], T) | bad4(xq[2], T) | bad4(xq[3], T), M16)) break;
          order();
        }
        STAMP(6);
#pragma unroll
        for (int i = 0; i < 16; ++i) xf[i] = hi2f(xq[i >> 2][i & 3]);
      }
      float sum = 0.f;
#pragma unroll
      for (int i = 0; i < 16; ++i) sum += xf[i];
      sum += dpp_ror8(sum);
      sum = x32_sum(x16_sum(sum));
      const float mw = sum * (1.f / 128.f);
      float sq = 0.f;
#pragma unroll
      for (int i = 0; i < 16; ++i) { float e = xf[i] - mw; sq += e * e; }
      sq += dpp_ror8(sq);
      sq = x32_sum(x16_sum(sq));
      if (lane < 8) lds_st2(L, L_ST + (lane * 8 + w) * 8, v2u{__float_as_uint(mw), __float_as_uint(sq)});
      wg_barrier();
      float ms[8], qs[8];
#pragma unroll
      for (int k = 0; k < 4; ++k) {
        v4u q = lds_ld4(L, L_ST + tok * 64 + k * 16);
        ms[2 * k] = __uint_as_float(q.x); qs[2 * k] = __uint_as_float(q.y);
        ms[2 * k + 1] = __uint_as_float(q.z); qs[2 * k + 1] = __uint_as_float(q.w);
      }
      float mean = 0.f;
#pragma unroll
      for (int k = 0; k < 8; ++k) mean += ms[k];
      mean *= 0.125f;
      float m2 = 0.f;
#pragma unroll
      for (int k = 0; k < 8; ++k) { float e = ms[k] - mean; m2 += qs[k] + 128.f * e * e; }
      const float rstd = 1.f / sqrtf(m2 * (1.f / 1024.f) + 1e-5f);
#pragma unroll
      for (int G = 0; G < 2; ++G)
#pragma unroll
        for (int d = 0; d < 4; ++d) {
          const int i = 8 * G + 2 * d;
          a[G][d] = pack2((xf[i] - mean) * rstd * cs[i] + sb[i], (xf[i + 1] - mean) * rstd * cs[i + 1] + sb[i + 1]);
        }
      tile_mma<2>(a, wb, f, lo8);
      float s = cross_wave(L, L_RED, f, w, lane);
      st1(ws, GU_OFF + ((w * 2 + mat) * 3584 + 64 * ct + lane) * 4, tag16(rbf_bits(s), T));
      STAMP(7);
    }
  }
}

}

extern "C" void kernel_launch(void* const* d_in, const int* in_sizes, int n_in, void* d_out, int out_size,
                              void* d_ws, size_t ws_size, hipStream_t stream) {
  Params p;
  p.x = (const u16*)d_in[0];
  p.kc = (const u16*)d_in[1];
  p.vc = (const u16*)d_in[2];
  p.wq = (const u16*)d_in[3];
  p.wo = (const u16*)d_in[4];
  p.wg = (const u16*)d_in[6];
  p.wu = (const u16*)d_in[7];
  p.wd = (const u16*)d_in[8];
  p.c = (const u16*)d_in[9];
  p.wm = (const u16*)d_in[10];
  p.out = (u16*)d_out;
  p.ws = (u32*)d_ws;
  fused_block<<<256, 512, 0, stream>>>(p);
}
```

```cpp
#include <hip/hip_runtime.h>
#include <stdint.h>

typedef unsigned short u16;
typedef unsigned int u32;
typedef u32 v4u __attribute__((ext_vector_type(4)));
typedef u32 v2u __attribute__((ext_vector_type(2)));
typedef float f32x4 __attribute__((ext_vector_type(4)));
typedef __bf16 bf16x8 __attribute__((ext_vector_type(8)));
typedef __amdgpu_buffer_rsrc_t rsrc_t;

namespace {

constexpr int W_M1 = 8, W_Q = 8, W_A = 6, W_O = 4, W_M2 = 4, W_G = 6, W_D = 4;
constexpr int G_KV = 100, G_O = 0, G_M2 = 0, G_G = 0, G_D = 0;
constexpr int POLL_SLEEP = 1, PROBE_SLEEP = 4, CODE_KB = 16;

constexpr int AUX_SC1 = 16, AUX_NT = 2, RSRC_W3 = 0x00020000;
constexpr u32 M6 = 0x3Fu, M16 = 0xFFFFu;
constexpr int SPIN_CAP = 1 << 16;

constexpr int M1P_OFF = 1024;
constexpr int QP_OFF = M1P_OFF + 131072;
constexpr int AO_OFF = QP_OFF + 1048576;
constexpr int AML_OFF = AO_OFF + 524288;
constexpr int OP_OFF = AML_OFF + 16384;
constexpr int MODT_OFF = OP_OFF + 1048576;
constexpr int MODD_OFF = MODT_OFF + 65536;
constexpr int MODF_OFF = MODD_OFF + 32768;
constexpr int X1_OFF = MODF_OFF + 1024;
constexpr int GU_OFF = X1_OFF + 32768;
constexpr int DP_OFF = GU_OFF + 229376;
constexpr int WS_BYTES = DP_OFF + 917504;

constexpr int L_VT = 0;
constexpr int L_RED = 0;
constexpr int L_PB = 65536;
constexpr int L_ML = L_PB + 8192;
constexpr int L_QS = L_ML + 512;
constexpr int L_ST = L_QS + 2176;
constexpr int L_SS = L_ST + 512;
constexpr int L_RED2 = L_SS + 2176;
constexpr int LDS_BYTES = L_RED2 + 17408;

struct Params {
  const u16 *x, *kc, *vc, *wq, *wo, *wg, *wu, *wd, *c, *wm;
  u16* out;
  u32* ws;
};

__device__ __forceinline__ float bf2f(u32 hi16_in_low) { return __uint_as_float(hi16_in_low << 16); }
__device__ __forceinline__ float hi2f(u32 w) { return __uint_as_float(w & 0xFFFF0000u); }
__device__ __forceinline__ float lo2f(u32 w) { return __uint_as_float(w << 16); }
typedef float f32x2 __attribute__((ext_vector_type(2)));
typedef __bf16 bf16x2 __attribute__((ext_vector_type(2)));
__device__ __forceinline__ u32 pack2(float lo, float hi) {
  return __builtin_bit_cast(u32, __builtin_convertvector(f32x2{lo, hi}, bf16x2));
}
__device__ __forceinline__ u32 rbf_bits(float f) { return pack2(0.f, f); }
__device__ __forceinline__ float rbf(float f) { return __uint_as_float(rbf_bits(f)); }
__device__ __forceinline__ float rcp(float x) { return __builtin_amdgcn_rcpf(x); }
__device__ __forceinline__ float silu_f(float x) { return x * rcp(1.f + __expf(-x)); }

__device__ __forceinline__ f32x4 mfma(v4u a, v4u b, f32x4 c) {
  return __builtin_amdgcn_mfma_f32_16x16x32_bf16(__builtin_bit_cast(bf16x8, a), __builtin_bit_cast(bf16x8, b), c, 0, 0, 0);
}
__device__ __forceinline__ rsrc_t mk_rsrc(const void* p, int bytes) {
  return __builtin_amdgcn_make_buffer_rsrc(const_cast<void*>(p), 0, bytes, RSRC_W3);
}
__device__ __forceinline__ v4u ld_nt(rsrc_t r, int voff, int soff) { return __builtin_amdgcn_raw_buffer_load_b128(r, voff, soff, AUX_NT); }
__device__ __forceinline__ v4u ld_pl(rsrc_t r, int voff, int soff) { return __builtin_amdgcn_raw_buffer_load_b128(r, voff, soff, 0); }
__device__ __forceinline__ v4u ld4(rsrc_t r, int off) { return __builtin_amdgcn_raw_buffer_load_b128(r, off, 0, AUX_SC1); }
__device__ __forceinline__ v2u ld2(rsrc_t r, int off) { return __builtin_amdgcn_raw_buffer_load_b64(r, off, 0, AUX_SC1); }
__device__ __forceinline__ u32 ld1(rsrc_t r, int off) { return __builtin_amdgcn_raw_buffer_load_b32(r, off, 0, AUX_SC1); }
__device__ __forceinline__ void st4(rsrc_t r, int off, v4u v) { __builtin_amdgcn_raw_buffer_store_b128(v, r, off, 0, AUX_SC1); }
__device__ __forceinline__ void st2(rsrc_t r, int off, v2u v) { __builtin_amdgcn_raw_buffer_store_b64(v, r, off, 0, AUX_SC1); }
__device__ __forceinline__ void st1(rsrc_t r, int off, u32 v) { __builtin_amdgcn_raw_buffer_store_b32(v, r, off, 0, AUX_SC1); }

__device__ __forceinline__ void wg_barrier() { asm volatile("s_waitcnt lgkmcnt(0)\n\ts_barrier" ::: "memory"); }
__device__ __forceinline__ void order() { asm volatile("" ::: "memory"); }
template <class T> __device__ __forceinline__ void pin(T& v) { asm volatile("" : "+v"(v)); }

__device__ __forceinline__ float dpp_ror8(float v) {
  return __int_as_float(__builtin_amdgcn_update_dpp(0, __float_as_int(v), 0x128, 0xf, 0xf, true));
}
template <int N> __device__ __forceinline__ float dpp_ror(float v) {
  return __int_as_float(__builtin_amdgcn_update_dpp(0, __float_as_int(v), 0x120 + N, 0xf, 0xf, true));
}
__device__ __forceinline__ float row16_sum(float v) {
  v += dpp_ror<8>(v); v += dpp_ror<4>(v); v += dpp_ror<2>(v); v += dpp_ror<1>(v);
  return v;
}
__device__ __forceinline__ float x16_sum(float v) {
  auto r = __builtin_amdgcn_permlane16_swap(__float_as_uint(v), __float_as_uint(v), false, false);
  return __uint_as_float(r[0]) + __uint_as_float(r[1]);
}
__device__ __forceinline__ float x32_sum(float v) {
  auto r = __builtin_amdgcn_permlane32_swap(__float_as_uint(v), __float_as_uint(v), false, false);
  return __uint_as_float(r[0]) + __uint_as_float(r[1]);
}
__device__ __forceinline__ float x16_max(float v) {
  auto r = __builtin_amdgcn_permlane16_swap(__float_as_uint(v), __float_as_uint(v), false, false);
  return fmaxf(__uint_as_float(r[0]), __uint_as_float(r[1]));
}
__device__ __forceinline__ float x32_max(float v) {
  auto r = __builtin_amdgcn_permlane32_swap(__float_as_uint(v), __float_as_uint(v), false, false);
  return fmaxf(__uint_as_float(r[0]), __uint_as_float(r[1]));
}
__device__ __forceinline__ float wave_sum(float v) { return x32_sum(x16_sum(row16_sum(v))); }

__device__ __forceinline__ u32 tag6(float v, u32 T) { return (__float_as_uint(v) & ~M6) | (T & M6); }
__device__ __forceinline__ u32 tag16(u32 bf_hi, u32 T) { return (bf_hi & ~M16) | (T & M16); }
__device__ __forceinline__ float untag6(u32 w) { return __uint_as_float(w & ~M6); }
__device__ __forceinline__ u32 bad4(v4u v, u32 T) { return (v.x ^ T) | (v.y ^ T) | (v.z ^ T) | (v.w ^ T); }
__device__ __forceinline__ u32 bad2(v2u v, u32 T) { return (v.x ^ T) | (v.y ^ T); }
__device__ __forceinline__ bool all_ok(u32 bad, u32 mask) { return __builtin_amdgcn_ballot_w64((bad & mask) != 0u) == 0ull; }
__device__ __forceinline__ void probe(rsrc_t ws, int off, u32 T, u32 mask) {
#pragma nounroll
  for (int s = 0; s < SPIN_CAP; ++s) {
    u32 v = ld1(ws, off);
    if (((__builtin_amdgcn_readfirstlane(v) ^ T) & mask) == 0u) break;
    __builtin_amdgcn_s_sleep(PROBE_SLEEP);
    order();
  }
}
__device__ __forceinline__ void gate(unsigned long long t0, int ticks) {
  if (ticks <= 0) return;
#pragma nounroll
  for (int s = 0; s < SPIN_CAP; ++s) {
    if (__builtin_amdgcn_s_memrealtime() - t0 >= (unsigned long long)ticks) break;
    __builtin_amdgcn_s_sleep(2);
  }
  order();
}
__device__ __forceinline__ v4u frag8(v4u a, v4u b) {
  v4u f;
  f.x = __builtin_amdgcn_perm(a.y, a.x, 0x07060302u);
  f.y = __builtin_amdgcn_perm(a.w, a.z, 0x07060302u);
  f.z = __builtin_amdgcn_perm(b.y, b.x, 0x07060302u);
  f.w = __builtin_amdgcn_perm(b.w, b.z, 0x07060302u);
  return f;
}

__device__ __forceinline__ void transpose8(const v4u* in, v4u* out) {
#pragma unroll
  for (int t = 0; t < 8; ++t) {
    const u32 sel = (t & 1) ? 0x07060302u : 0x05040100u;
#pragma unroll
    for (int d = 0; d < 4; ++d) out[t][d] = __builtin_amdgcn_perm(in[2 * d + 1][t >> 1], in[2 * d][t >> 1], sel);
  }
}

template <int N> __device__ __forceinline__ void wait_vm() { asm volatile("s_waitcnt vmcnt(%0)" ::"n"(N) : "memory"); }
template <int NG, int WIN>
__device__ __forceinline__ void load_tile(v4u* w, rsrc_t r, int voff, int row_bytes) {
#pragma unroll
  for (int G = 0; G < NG; ++G)
#pragma unroll
    for (int i = 0; i < 8; ++i) {
      if (G * 8 + i >= WIN) wait_vm<WIN - 1>();
      w[G * 8 + i] = ld_nt(r, voff, (64 * G + i) * row_bytes);
    }
}
template <int NG>
__device__ __forceinline__ void prep_tile(const v4u* w, v4u* b) {
#pragma unroll
  for (int G = 0; G < NG; ++G) transpose8(w + 8 * G, b + 8 * G);
#pragma unroll
  for (int i = 0; i < NG * 8; ++i) pin(b[i]);
}
template <int NG>
__device__ __forceinline__ void tile_mma(const v4u* a, const v4u* b, f32x4* f, bool lo8) {
  f32x4 acc[8];
#pragma unroll
  for (int t = 0; t < 8; ++t) {
    acc[t] = mfma(a[0], b[t], f32x4{0.f, 0.f, 0.f, 0.f});
    if (NG == 2) acc[t] = mfma(a[1], b[8 + t], acc[t]);
  }
  float pa[4][4], pb[4][4];
#pragma unroll
  for (int t = 0; t < 4; ++t)
#pragma unroll
    for (int r = 0; r < 4; ++r) {
      auto s = __builtin_amdgcn_permlane32_swap(__float_as_uint(acc[t][r]), __float_as_uint(acc[t + 4][r]), false, false);
      pa[t][r] = __uint_as_float(s[0]);
      pb[t][r] = __uint_as_float(s[1]);
    }
#pragma unroll
  for (int rp = 0; rp < 2; ++rp)
#pragma unroll
    for (int t = 0; t < 4; ++t) {
      float x = lo8 ? pa[t][2 * rp] : pb[t][2 * rp + 1];
      float y = lo8 ? pa[t][2 * rp + 1] : pb[t][2 * rp];
      f[rp][t] = x + dpp_ror8(y);
    }
}

typedef __attribute__((address_space(3))) unsigned char lds_t;
__device__ __forceinline__ void lds_st4(lds_t* L, int off, v4u v) { *(__attribute__((address_space(3))) v4u*)(L + off) = v; }
__device__ __forceinline__ v4u lds_ld4(lds_t* L, int off) { return *(__attribute__((address_space(3))) v4u*)(L + off); }
__device__ __forceinline__ void lds_st2(lds_t* L, int off, v2u v) { *(__attribute__((address_space(3))) v2u*)(L + off) = v; }
__device__ __forceinline__ v2u lds_ld2(lds_t* L, int off) { return *(__attribute__((address_space(3))) v2u*)(L + off); }
__device__ __forceinline__ void lds_st1(lds_t* L, int off, u32 v) { *(__attribute__((address_space(3))) u32*)(L + off) = v; }
__device__ __forceinline__ float lds_ldf(lds_t* L, int off) { return *(__attribute__((address_space(3))) float*)(L + off); }

__device__ __forceinline__ float cross_wave(lds_t* L, int red, const f32x4* f, int w, int lane) {
  const int q4 = lane >> 4, hj = (lane >> 3) & 1, c = lane & 7;
  const int base = red + w * 2176 + (4 * (q4 & 1) + hj) * 272 + (8 * c + 4 * (q4 >> 1)) * 4;
  lds_st4(L, base, __builtin_bit_cast(v4u, f[0]));
  lds_st4(L, base + 544, __builtin_bit_cast(v4u, f[1]));
  wg_barrier();
  float s = 0.f;
#pragma unroll
  for (int ww = 0; ww < 8; ++ww) s += lds_ldf(L, red + ww * 2176 + w * 272 + lane * 4);
  return s;
}
__device__ __forceinline__ void publish_part(rsrc_t ws, int off, int row_bytes, const f32x4* f, u32 T, int lane) {
  const int q4 = lane >> 4, hj = (lane >> 3) & 1, c = lane & 7;
  const int o = off + (4 * (q4 & 1) + hj) * row_bytes + (8 * c + 4 * (q4 >> 1)) * 4;
#pragma unroll
  for (int rp = 0; rp < 2; ++rp)
    st4(ws, o + 2 * rp * row_bytes, v4u{tag6(f[rp][0], T), tag6(f[rp][1], T), tag6(f[rp][2], T), tag6(f[rp][3], T)});
}
__device__ __forceinline__ v4u silu_frag(v4u cv) {
  v4u f;
#pragma unroll
  for (int d = 0; d < 4; ++d) f[d] = pack2(silu_f(lo2f(cv[d])), silu_f(hi2f(cv[d])));
  return f;
}

__device__ __forceinline__ u32 epoch(u32 raw) {
  pin(raw);
  return __builtin_amdgcn_readfirstlane(raw) + 1u;
}
__device__ __forceinline__ void poll_pause() { __builtin_amdgcn_s_sleep(POLL_SLEEP); order(); }

template <int NP>
__device__ __forceinline__ f32x4 reduce_parts(rsrc_t ws, int off, u32 T, int lane) {
  const int hl = lane & 15;
  const int o0 = off + hl * 32768, o1 = off + (hl + 16 < NP ? hl + 16 : hl) * 32768;
  v4u a, b;
#pragma nounroll
  for (int s = 0; s < SPIN_CAP; ++s) {
    a = ld4(ws, o0);
    b = ld4(ws, o1);
    if (all_ok(bad4(a, T) | bad4(b, T), M6)) break;
    poll_pause();
  }
  f32x4 r;
#pragma unroll
  for (int k = 0; k < 4; ++k) {
    float v = untag6(a[k]) + (hl + 16 < NP ? untag6(b[k]) : 0.f);
    r[k] = row16_sum(v);
  }
  return r;
}

__global__ void __launch_bounds__(512, 2) fused_block(Params p) {
  __shared__ __attribute__((aligned(16))) unsigned char lds_raw[LDS_BYTES];
  lds_t* L = (lds_t*)lds_raw;
  const int tid = threadIdx.x, lane = tid & 63, w = __builtin_amdgcn_readfirstlane(tid >> 6);
  const int bid = blockIdx.x, xl = bid & 7, u = bid >> 3;
  const int g = lane >> 4, rho = (lane >> 3) & 1, c = lane & 7, tok = lane & 7;
  const bool lo8 = rho == 0;
  const rsrc_t ws = mk_rsrc(p.ws, WS_BYTES);
  const unsigned long long t_start = __builtin_amdgcn_s_memrealtime();
  const u32 t_raw = __builtin_amdgcn_raw_buffer_load_b32(ws, 0, 0, 0);
  const int krow = 32 * rho + 8 * g;
  u32 code_pf;
  {
    const unsigned long long pc = __builtin_amdgcn_s_getpc() & ~255ull;
    const rsrc_t rcode = mk_rsrc((const void*)pc, CODE_KB * 1024);
    int piece = (u & 1) * 8 + w + (u & 2) * 8;
    if (piece >= CODE_KB) piece -= 16;
    code_pf = __builtin_amdgcn_raw_buffer_load_b32(rcode, piece * 1024 + lane * 16, 0, 0);
  }
  v4u wr[16], wb[16], a[2];
  f32x4 f[2];

  if (u < 8) {
    const int j = u + 8 * (xl & 1), pq = xl >> 1;
    const int ct = (pq >> 1) * 16 + j, k0 = 512 * (pq & 1) + 64 * w;
    const rsrc_t rc = mk_rsrc(p.c, 8192 * 2);
    v4u cv = ld_pl(rc, (tok * 1024 + k0 + krow) * 2, 0);
    const rsrc_t rm = mk_rsrc(p.wm, 1024 * 6144 * 2);
    load_tile<1, W_M1>(wr, rm, ((k0 + krow) * 6144 + 64 * ct + 8 * c) * 2, 6144 * 2);
    a[0] = silu_frag(cv);
    pin(a[0]);
    prep_tile<1>(wr, wb);
    tile_mma<1>(a, wb, f, lo8);
    float s = cross_wave(L, L_RED2, f, w, lane);
    const u32 T = epoch(t_raw);
    st1(ws, M1P_OFF + ((pq * 8 + w) * 1024 + 64 * j + lane) * 4, tag6(s, T));
  }
  asm volatile("" ::"v"(code_pf));
  order();

  if (u >= 8 && u < 16) {
    const int t = xl * 8 + (u - 8), kc = t & 7, nc = t >> 3, arr = xl >> 1;
    const rsrc_t rc = mk_rsrc(p.c, 8192 * 2), rx = mk_rsrc(p.x, 8192 * 2);
    v4u x0 = ld_pl(rx, (w * 1024 + lane * 16) * 2, 0), x1v = ld_pl(rx, (w * 1024 + lane * 16 + 8) * 2, 0);
    const u32 xs = __builtin_amdgcn_raw_buffer_load_b32(rx, (w * 1024 + 128 * kc + 2 * lane) * 2, 0, 0);
    const int rcol = 16 * t + 4 * (lane >> 4);
    v2u xv = __builtin_amdgcn_raw_buffer_load_b64(rx, (w * 1024 + rcol) * 2, 0, 0);
    v4u cv0 = ld_pl(rc, (tok * 1024 + 128 * w + krow) * 2, 0), cv1 = ld_pl(rc, (tok * 1024 + 128 * w + 64 + krow) * 2, 0);
    const rsrc_t rq = mk_rsrc(p.wq, 1024 * 4096 * 2);
    load_tile<2, W_Q>(wr, rq, ((128 * kc + krow) * 4096 + 512 * nc + 64 * w + 8 * c) * 2, 4096 * 2);
    v4u ac[2];
    ac[0] = silu_frag(cv0); ac[1] = silu_frag(cv1);
    pin(ac[0]); pin(ac[1]);
    float sum = 0.f;
#pragma unroll
    for (int d = 0; d < 4; ++d) sum += (lo2f(x0[d]) + hi2f(x0[d])) + (lo2f(x1v[d]) + hi2f(x1v[d]));
    const float mean = wave_sum(sum) * (1.f / 1024.f);
    float sq = 0.f;
#pragma unroll
    for (int d = 0; d < 4; ++d) {
      float e0 = lo2f(x0[d]) - mean, e1 = hi2f(x0[d]) - mean, e2 = lo2f(x1v[d]) - mean, e3 = hi2f(x1v[d]) - mean;
      sq += (e0 * e0 + e1 * e1) + (e2 * e2 + e3 * e3);
    }
    const float var = rbf(wave_sum(sq) * (1.f / 1024.f));
    const float sd = rbf(__builtin_amdgcn_sqrtf(rbf(var + 1e-5f)));
    const float isd = rcp(sd);
    float n0 = rbf(rbf(lo2f(xs) - rbf(mean)) * isd), n1 = rbf(rbf(hi2f(xs) - rbf(mean)) * isd);
    pin(n0); pin(n1);
    prep_tile<2>(wr, wb);
    const u32 T = epoch(t_raw);
    {
      const int mo = M1P_OFF + (w * 1024 + 128 * kc + 2 * lane) * 4;
      v2u m0, m1, m2, m3;
#pragma nounroll
      for (int sp = 0; sp < SPIN_CAP; ++sp) {
        m0 = ld2(ws, mo); m1 = ld2(ws, mo + 32768); m2 = ld2(ws, mo + 65536); m3 = ld2(ws, mo + 98304);
        if (all_ok(bad2(m0, T) | bad2(m1, T) | bad2(m2, T) | bad2(m3, T), M6)) break;
        poll_pause();
      }
      const float sa0 = rbf(untag6(m0.x) + untag6(m1.x)), ca0 = rbf(untag6(m2.x) + untag6(m3.x));
      const float sa1 = rbf(untag6(m0.y) + untag6(m1.y)), ca1 = rbf(untag6(m2.y) + untag6(m3.y));
      lds_st1(L, L_QS + w * 272 + lane * 4, pack2(rbf(n0 * rbf(1.f + ca0)) + sa0, rbf(n1 * rbf(1.f + ca1)) + sa1));
      wg_barrier();
      a[0] = lds_ld4(L, L_QS + tok * 272 + krow * 2);
      a[1] = lds_ld4(L, L_QS + tok * 272 + (64 + krow) * 2);
    }
    tile_mma<2>(a, wb, f, lo8);
    publish_part(ws, QP_OFF + kc * 131072 + (512 * nc + 64 * w) * 4, 16384, f, T, lane);
    order();
    gate(t_start, G_M2);
    const rsrc_t rm = mk_rsrc(p.wm, 1024 * 6144 * 2);
    load_tile<2, W_M2>(wr, rm, ((128 * w + krow) * 6144 + 2048 + 64 * t + 8 * c) * 2, 6144 * 2);
    prep_tile<2>(wr, wb);
    tile_mma<2>(ac, wb, f, lo8);
    {
      const float s = cross_wave(L, L_RED2, f, w, lane);
      const u32 sb = rbf_bits(s);
      const int j = t & 15;
      if (arr == 0 || arr == 3) {
        st1(ws, MODT_OFF + (((arr == 3) * 8 + w) * 1024 + 64 * j + lane) * 4, tag16(sb, T));
      } else {
        const u32 other = __builtin_amdgcn_update_dpp(0, (int)sb, 0xB1, 0xf, 0xf, true);
        if ((lane & 1) == 0) st1(ws, MODD_OFF + (((arr - 1) * 8 + w) * 1024 + 64 * j + lane) * 2, (sb >> 16) | other);
        wait_vm<0>();
        if (lane == 0) st1(ws, MODF_OFF + (((arr - 1) * 16 + j) * 8 + w) * 4, T);
      }
    }
    v4u gav, gbv;
#pragma nounroll
    for (int sp = 0; sp < SPIN_CAP; ++sp) {
      gav = ld4(ws, MODT_OFF + (w * 1024 + rcol) * 4);
      if (all_ok(bad4(gav, T), M16)) break;
      poll_pause();
    }
    f32x4 at = reduce_parts<32>(ws, OP_OFF + (w * 1024 + rcol) * 4, T, lane);
    float x1[4];
    x1[0] = rbf(lo2f(xv.x) + rbf(hi2f(gav.x) * rbf(at[0])));
    x1[1] = rbf(hi2f(xv.x) + rbf(hi2f(gav.y) * rbf(at[1])));
    x1[2] = rbf(lo2f(xv.y) + rbf(hi2f(gav.z) * rbf(at[2])));
    x1[3] = rbf(hi2f(xv.y) + rbf(hi2f(gav.w) * rbf(at[3])));
    if ((lane & 15) == 0)
      st4(ws, X1_OFF + (w * 1024 + rcol) * 4,
          v4u{tag16(__float_as_uint(x1[0]), T), tag16(__float_as_uint(x1[1]), T), tag16(__float_as_uint(x1[2]), T), tag16(__float_as_uint(x1[3]), T)});
#pragma nounroll
    for (int sp = 0; sp < SPIN_CAP; ++sp) {
      gbv = ld4(ws, MODT_OFF + ((8 + w) * 1024 + rcol) * 4);
      if (all_ok(bad4(gbv, T), M16)) break;
      poll_pause();
    }
    probe(ws, GU_OFF + (w * 7168 + 56 * t) * 4, T, M16);
    f32x4 dn = reduce_parts<28>(ws, DP_OFF + (w * 1024 + rcol) * 4, T, lane);
    if ((lane & 15) == 0) {
      v2u o;
      o.x = pack2(x1[0] + rbf(hi2f(gbv.x) * rbf(dn[0])), x1[1] + rbf(hi2f(gbv.y) * rbf(dn[1])));
      o.y = pack2(x1[2] + rbf(hi2f(gbv.z) * rbf(dn[2])), x1[3] + rbf(hi2f(gbv.w) * rbf(dn[3])));
      *(v2u*)(p.out + w * 1024 + rcol) = o;
    }
    if (t == 0 && tid == 0) p.ws[0] = T;
  } else if (u >= 16) {
    const int yi = xl * 16 + (u - 16);
    gate(t_start, G_KV);
    const u32 T = epoch(t_raw);
    {
      const int pi = yi >> 2, sp_ = yi & 3, b = pi >> 2, hkv = pi & 3;
      const int n = lane & 15;
      const int kbase = (pi * 4096 + 3072 + 256 * sp_ + 32 * w) * 256;
      const rsrc_t rk = mk_rsrc(p.kc, 8 * 4 * 4096 * 128 * 2), rv = mk_rsrc(p.vc, 8 * 4 * 4096 * 128 * 2);
#pragma unroll
      for (int i = 0; i < 8; ++i) {
        if (i >= W_A) wait_vm<W_A - 1>();
        wr[8 + i] = ld_nt(rv, kbase + 4 * g * 256 + n * 16, (16 * (i >> 2) + (i & 3)) * 256);
      }
#pragma unroll
      for (int i = 0; i < 8; ++i) {
        if (8 + i >= W_A) wait_vm<W_A - 1>();
        wr[i] = ld_nt(rk, kbase + n * 256 + g * 16, (i >> 2) * 4096 + (i & 3) * 64);
      }
      transpose8(wr + 8, wb);
#pragma unroll
      for (int t = 0; t < 8; ++t) lds_st4(L, L_VT + ((w * 4 + g) * 128 + t * 16 + (n ^ (2 * t))) * 16, wb[t]);
      wg_barrier();
#pragma unroll
      for (int v = 0; v < 8; ++v) wb[v] = lds_ld4(L, L_VT + ((v * 4 + g) * 128 + (n & 7) * 16 + ((2 * w + (n >> 3)) ^ (2 * (n & 7)))) * 16);
#pragma unroll
      for (int i = 0; i < 8; ++i) { pin(wb[i]); pin(wr[i]); }
      {
        const int qo = QP_OFF + (b * 4096 + 1024 * hkv + 2 * tid) * 4;
        v2u qv[8];
#pragma nounroll
        for (int sp = 0; sp < SPIN_CAP; ++sp) {
          u32 bad = 0;
#pragma unroll
          for (int k = 0; k < 8; ++k) { qv[k] = ld2(ws, qo + k * 131072); }
#pragma unroll
          for (int k = 0; k < 8; ++k) bad |= bad2(qv[k], T);
          if (all_ok(bad, M6)) break;
          poll_pause();
        }
        float q0 = 0.f, q1 = 0.f;
#pragma unroll
        for (int k = 0; k < 8; ++k) { q0 += untag6(qv[k].x); q1 += untag6(qv[k].y); }
        lds_st1(L, L_QS + w * 272 + lane * 4, pack2(q0, q1));
      }
      wg_barrier();
      f32x4 sc[2];
      {
        v4u qf[4];
#pragma unroll
        for (int kk = 0; kk < 4; ++kk) qf[kk] = lds_ld4(L, L_QS + (n & 7) * 272 + kk * 64 + g * 16);
#pragma unroll
        for (int T2 = 0; T2 < 2; ++T2) {
          sc[T2] = mfma(wr[T2 * 4], qf[0], f32x4{0.f, 0.f, 0.f, 0.f});
#pragma unroll
          for (int kk = 1; kk < 4; ++kk) sc[T2] = mfma(wr[T2 * 4 + kk], qf[kk], sc[T2]);
        }
      }
      float mx = -INFINITY;
#pragma unroll
      for (int T2 = 0; T2 < 2; ++T2)
#pragma unroll
        for (int r = 0; r < 4; ++r) { sc[T2][r] = rbf(rbf(sc[T2][r]) * 0.08838834764831845f); mx = fmaxf(mx, sc[T2][r]); }
      mx = x32_max(x16_max(mx));
      float ls = 0.f;
#pragma unroll
      for (int T2 = 0; T2 < 2; ++T2)
#pragma unroll
        for (int r = 0; r < 4; ++r) { sc[T2][r] = __expf(sc[T2][r] - mx); ls += sc[T2][r]; }
      ls = x32_sum(x16_sum(ls));
      lds_st4(L, L_PB + (w * 64 + lane) * 16,
              v4u{pack2(sc[0][0], sc[0][1]), pack2(sc[0][2], sc[0][3]), pack2(sc[1][0], sc[1][1]), pack2(sc[1][2], sc[1][3])});
      if (lane < 8) lds_st2(L, L_ML + (lane * 8 + w) * 8, v2u{__float_as_uint(mx), __float_as_uint(ls)});
      wg_barrier();
      float mv[8], lv[8];
#pragma unroll
      for (int k = 0; k < 4; ++k) {
        v4u q = lds_ld4(L, L_ML + (n & 7) * 64 + k * 16);
        mv[2 * k] = __uint_as_float(q.x); lv[2 * k] = __uint_as_float(q.y);
        mv[2 * k + 1] = __uint_as_float(q.z); lv[2 * k + 1] = __uint_as_float(q.w);
      }
      float M = mv[0];
#pragma unroll
      for (int v = 1; v < 8; ++v) M = fmaxf(M, mv[v]);
      float Ls = 0.f;
      f32x4 o = {0.f, 0.f, 0.f, 0.f};
#pragma unroll
      for (int v = 0; v < 8; ++v) {
        const float e = __expf(mv[v] - M);
        Ls += e * lv[v];
        f32x4 d = mfma(wb[v], lds_ld4(L, L_PB + (v * 64 + lane) * 16), f32x4{0.f, 0.f, 0.f, 0.f});
        o += e * d;
      }
      if (n < 8) {
        const int hq = b * 32 + 8 * hkv + n;
        st4(ws, AO_OFF + ((hq * 4 + sp_) * 128 + 16 * w + 4 * g) * 4, v4u{tag6(o[0], T), tag6(o[1], T), tag6(o[2], T), tag6(o[3], T)});
        if (w == 0 && g == 0) {
          const u32 mt = tag16(__float_as_uint(M), T), lt = tag6(Ls, T);
          st4(ws, AML_OFF + (hq * 4 + sp_) * 16, v4u{mt, lt, mt, lt});
        }
      }
    }
    order();
    if (u < 30) {
      gate(t_start, G_G);
      const int gt = xl * 14 + (u - 16), mat = gt >= 56, ct = gt - 56 * mat;
      const rsrc_t rg = mk_rsrc(mat ? p.wu : p.wg, 1024 * 3584 * 2);
      load_tile<2, W_G>(wr, rg, ((128 * w + krow) * 3584 + 64 * ct + 8 * c) * 2, 3584 * 2);
      {
        const int fo = MODF_OFF + ((((lane >> 4) & 1) * 16 + 2 * w) * 8 + (lane & 15)) * 4;
#pragma nounroll
        for (int sp = 0; sp < SPIN_CAP; ++sp) {
          const u32 fl = ld1(ws, fo);
          if (all_ok(fl ^ T, 0xFFFFFFFFu)) break;
          poll_pause();
        }
      }
      order();
      const int so = MODD_OFF + (tok * 1024 + 128 * w + krow) * 2;
      v4u s0 = ld4(ws, so), s1 = ld4(ws, so + 128), k0 = ld4(ws, so + 16384), k1 = ld4(ws, so + 16384 + 128);
      prep_tile<2>(wr, wb);
      float cs[16], sb[16];
#pragma unroll
      for (int d = 0; d < 4; ++d) {
        sb[2 * d] = lo2f(s0[d]); sb[2 * d + 1] = hi2f(s0[d]); sb[8 + 2 * d] = lo2f(s1[d]); sb[8 + 2 * d + 1] = hi2f(s1[d]);
        cs[2 * d] = rbf(1.f + lo2f(k0[d])); cs[2 * d + 1] = rbf(1.f + hi2f(k0[d]));
        cs[8 + 2 * d] = rbf(1.f + lo2f(k1[d])); cs[8 + 2 * d + 1] = rbf(1.f + hi2f(k1[d]));
      }
#pragma unroll
      for (int i = 0; i < 16; ++i) { pin(sb[i]); pin(cs[i]); }
      float xf[16];
      {
        const int xo = X1_OFF + (tok * 1024 + 128 * w + krow) * 4;
        v4u xq[4];
#pragma nounroll
        for (int sp = 0; sp < SPIN_CAP; ++sp) {
          xq[0] = ld4(ws, xo); xq[1] = ld4(ws, xo + 16); xq[2] = ld4(ws, xo + 256); xq[3] = ld4(ws, xo + 272);
          if (all_ok(bad4(xq[0], T) | bad4(xq[1], T) | bad4(xq[2], T) | bad4(xq[3], T), M16)) break;
          poll_pause();
        }
#pragma unroll
        for (int i = 0; i < 16; ++i) xf[i] = hi2f(xq[i >> 2][i & 3]);
      }
      float sum = 0.f;
#pragma unroll
      for (int i = 0; i < 16; ++i) sum += xf[i];
      sum += dpp_ror8(sum);
      sum = x32_sum(x16_sum(sum));
      const float mw = sum * (1.f / 128.f);
      float sq = 0.f;
#pragma unroll
      for (int i = 0; i < 16; ++i) { float e = xf[i] - mw; sq += e * e; }
      sq += dpp_ror8(sq);
      sq = x32_sum(x16_sum(sq));
      if (lane < 8) lds_st2(L, L_ST + (lane * 8 + w) * 8, v2u{__float_as_uint(mw), __float_as_uint(sq)});
      wg_barrier();
      float ms[8], qs[8];
#pragma unroll
      for (int k = 0; k < 4; ++k) {
        v4u q = lds_ld4(L, L_ST + tok * 64 + k * 16);
        ms[2 * k] = __uint_as_float(q.x); qs[2 * k] = __uint_as_float(q.y);
        ms[2 * k + 1] = __uint_as_float(q.z); qs[2 * k + 1] = __uint_as_float(q.w);
      }
      float mean = 0.f;
#pragma unroll
      for (int k = 0; k < 8; ++k) mean += ms[k];
      mean *= 0.125f;
      float m2 = 0.f;
#pragma unroll
      for (int k = 0; k < 8; ++k) { float e = ms[k] - mean; m2 += qs[k] + 128.f * e * e; }
      const float sd2 = rbf(__builtin_amdgcn_sqrtf(rbf(rbf(m2 * (1.f / 1024.f)) + 1e-5f)));
      const float rstd = rcp(sd2), mb = rbf(mean);
#pragma unroll
      for (int G = 0; G < 2; ++G)
#pragma unroll
        for (int d = 0; d < 4; ++d) {
          const int i = 8 * G + 2 * d;
          a[G][d] = pack2((xf[i] - mb) * rstd * cs[i] + sb[i], (xf[i + 1] - mb) * rstd * cs[i + 1] + sb[i + 1]);
        }
      tile_mma<2>(a, wb, f, lo8);
      float s = cross_wave(L, L_RED2, f, w, lane);
      st1(ws, GU_OFF + ((w * 2 + mat) * 3584 + 64 * ct + lane) * 4, tag16(rbf_bits(s), T));
    }
  } else {
    const int li = u;
    gate(t_start, G_O);
    const u32 T = epoch(t_raw);
    {
      const int zi = xl * 8 + li;
      const int hh = zi >> 1, half = zi & 1;
      const rsrc_t ro = mk_rsrc(p.wo, 4096 * 1024 * 2);
      load_tile<2, W_O>(wr, ro, ((128 * hh + krow) * 1024 + 512 * half + 64 * w + 8 * c) * 2, 1024 * 2);
      prep_tile<2>(wr, wb);
      const int ao = AO_OFF + (((w * 32 + hh) * 4) * 128 + 2 * lane) * 4, ml = AML_OFF + (w * 32 + hh) * 64;
      v2u o0, o1, o2, o3; v4u ma, mb, mc, md;
#pragma nounroll
      for (int sp = 0; sp < SPIN_CAP; ++sp) {
        o0 = ld2(ws, ao); o1 = ld2(ws, ao + 512); o2 = ld2(ws, ao + 1024); o3 = ld2(ws, ao + 1536);
        ma = ld4(ws, ml); mb = ld4(ws, ml + 16); mc = ld4(ws, ml + 32); md = ld4(ws, ml + 48);
        const u32 bm = (ma.x ^ T) | (ma.z ^ T) | (mb.x ^ T) | (mb.z ^ T) | (mc.x ^ T) | (mc.z ^ T) | (md.x ^ T) | (md.z ^ T);
        const u32 bl = (ma.y ^ T) | (ma.w ^ T) | (mb.y ^ T) | (mb.w ^ T) | (mc.y ^ T) | (mc.w ^ T) | (md.y ^ T) | (md.w ^ T);
        if (all_ok(((bad2(o0, T) | bad2(o1, T) | bad2(o2, T) | bad2(o3, T) | bl) & M6) | (bm & M16), M16)) break;
        poll_pause();
      }
      const float m0 = hi2f(ma.x), m1 = hi2f(mb.x), m2 = hi2f(mc.x), m3 = hi2f(md.x);
      const float M = fmaxf(fmaxf(m0, m1), fmaxf(m2, m3));
      const float e0 = __expf(m0 - M), e1 = __expf(m1 - M), e2 = __expf(m2 - M), e3 = __expf(m3 - M);
      const float inv = rcp(e0 * untag6(ma.y) + e1 * untag6(mb.y) + e2 * untag6(mc.y) + e3 * untag6(md.y));
      const float oa = (e0 * untag6(o0.x) + e1 * untag6(o1.x) + e2 * untag6(o2.x) + e3 * untag6(o3.x)) * inv;
      const float ob = (e0 * untag6(o0.y) + e1 * untag6(o1.y) + e2 * untag6(o2.y) + e3 * untag6(o3.y)) * inv;
      lds_st1(L, L_QS + w * 272 + lane * 4, pack2(oa, ob));
      wg_barrier();
      a[0] = lds_ld4(L, L_QS + tok * 272 + krow * 2);
      a[1] = lds_ld4(L, L_QS + tok * 272 + (64 + krow) * 2);
      tile_mma<2>(a, wb, f, lo8);
      publish_part(ws, OP_OFF + hh * 32768 + (512 * half + 64 * w) * 4, 4096, f, T, lane);
    }
    order();
    if (li < 7) {
      gate(t_start, G_D);
      const int di = xl * 7 + li;
      const int ku = di >> 1, half = di & 1;
      const rsrc_t rd = mk_rsrc(p.wd, 3584 * 1024 * 2);
      load_tile<2, W_D>(wr, rd, ((128 * ku + krow) * 1024 + 512 * half + 64 * w + 8 * c) * 2, 1024 * 2);
      prep_tile<2>(wr, wb);
      const int go = GU_OFF + (w * 7168 + 128 * ku + 2 * lane) * 4;
      v2u gg, uu;
#pragma nounroll
      for (int sp = 0; sp < SPIN_CAP; ++sp) {
        gg = ld2(ws, go); uu = ld2(ws, go + 3584 * 4);
        if (all_ok(bad2(gg, T) | bad2(uu, T), M16)) break;
        poll_pause();
      }
      const float s0 = rbf(silu_f(hi2f(gg.x))) * hi2f(uu.x), s1 = rbf(silu_f(hi2f(gg.y))) * hi2f(uu.y);
      lds_st1(L, L_SS + w * 272 + lane * 4, pack2(s0, s1));
      wg_barrier();
      a[0] = lds_ld4(L, L_SS + tok * 272 + krow * 2);
      a[1] = lds_ld4(L, L_SS + tok * 272 + (64 + krow) * 2);
      tile_mma<2>(a, wb, f, lo8);
      publish_part(ws, DP_OFF + ku * 32768 + (512 * half + 64 * w) * 4, 4096, f, T, lane);
    }
  }
}

}

extern "C" void kernel_launch(void* const* d_in, const int* in_sizes, int n_in, void* d_out, int out_size,
                              void* d_ws, size_t ws_size, hipStream_t stream) {
  Params p;
  p.x = (const u16*)d_in[0];
  p.kc = (const u16*)d_in[1];
  p.vc = (const u16*)d_in[2];
  p.wq = (const u16*)d_in[3];
  p.wo = (const u16*)d_in[4];
  p.wg = (const u16*)d_in[6];
  p.wu = (const u16*)d_in[7];
  p.wd = (const u16*)d_in[8];
  p.c = (const u16*)d_in[9];
  p.wm = (const u16*)d_in[10];
  p.out = (u16*)d_out;
  p.ws = (u32*)d_ws;
  fused_block<<<256, 512, 0, stream>>>(p);
}
```
